```python
import torch

def _softmax(x, axis=-1):
    return torch.softmax(x, dim=axis)
def _gelu(x):
    return torch.nn.functional.gelu(x, approximate="tanh")
def _silu(x):
    return torch.nn.functional.silu(x)
def _sigmoid(x):
    return torch.sigmoid(x)
def _layernorm(x, axis=-1, eps=1e-5):
    m = x.mean(dim=axis, keepdim=True)
    v = x.var(dim=axis, keepdim=True, unbiased=False)
    return (x - m) / torch.sqrt(v + eps)
def _scatter_add(tgt, idx, src, axis=0):
    return tgt.clone().index_add_(axis, idx, src)
def _segment_sum(src, seg, n):
    out = torch.zeros((n,) + src.shape[1:], dtype=src.dtype)
    return out.index_add_(0, seg, src)
def _pad(x, pw):
    flat = tuple(v for pair in reversed(pw) for v in pair)
    return torch.nn.functional.pad(x, flat)

kernel_name = 'standalone__mha_block_cached__near_ridge'

def setup_inputs():

    _w = (lambda a: (lambda t: t.to(torch.bfloat16) if t.is_floating_point() else t)(torch.as_tensor(a)))
    B, Sq, D = 8, 64, 1024
    H, Sk, Dh = 32, 64, 128
    return {
        "x": _w((lambda *s: torch.randn(*s, dtype=torch.bfloat16) * 0.8)(B, Sq, D)),
        "k_cache": _w((lambda *s: torch.randn(*s, dtype=torch.bfloat16) * 0.8)(B, H, Sk, Dh)),
        "v_cache": _w((lambda *s: torch.randn(*s, dtype=torch.bfloat16) * 0.05)(B, H, Sk, Dh)),
        "wq": _w((lambda *s: torch.randn(*s, dtype=torch.bfloat16) * 0.8)(D, H * Dh) * (D ** -0.5)),
        "wo": _w((lambda *s: torch.randn(*s, dtype=torch.bfloat16) * 0.05)(H * Dh, D) * ((H * Dh) ** -0.5)),
    }

def reference(x, k_cache, v_cache, wq, wo):
    B, Sq, D = x.shape
    H, Dh = 32, 128
    q = (lambda x, *d: x.permute(*d))((x @ wq).reshape(B, Sq, H, Dh), 0, 2, 1, 3)
    scores = torch.einsum("bhqd,bhkd->bhqk", q, k_cache) * (Dh ** -0.5)
    p = _softmax(scores, axis=-1)
    o = torch.einsum("bhqk,bhkd->bhqd", p, v_cache)
    return (lambda x, *d: x.permute(*d))(o, 0, 2, 1, 3).reshape(B, Sq, H * Dh) @ wo
```

```cpp
#include <hip/hip_runtime.h>
#include <hip/hip_bf16.h>
#include <stdint.h>

typedef __attribute__((ext_vector_type(8))) short bf16x8;
typedef __attribute__((ext_vector_type(4))) short s16x4;
typedef __attribute__((ext_vector_type(4))) float f32x4;
typedef __attribute__((ext_vector_type(4))) unsigned u32x4;
typedef __attribute__((ext_vector_type(2))) unsigned u32x2;
typedef __attribute__((address_space(3))) s16x4* lds_tr_ptr;

#define DI __device__ __forceinline__

constexpr int cB = 8, cSQ = 64, cD = 1024, cH = 32, cSK = 64, cDH = 128;
constexpr int cHD = cH * cDH;

DI unsigned pack_bf16(float lo, float hi) {
  __hip_bfloat16 a = __float2bfloat16(lo), b = __float2bfloat16(hi);
  unsigned short ua = __builtin_bit_cast(unsigned short, a), ub = __builtin_bit_cast(unsigned short, b);
  return (unsigned)ua | ((unsigned)ub << 16);
}

DI s16x4 tr_read(const char* p) {
  return __builtin_amdgcn_ds_read_tr16_b64_v4i16((lds_tr_ptr)(p));
}

DI bf16x8 cat8(s16x4 lo, s16x4 hi) { return __builtin_shufflevector(lo, hi, 0, 1, 2, 3, 4, 5, 6, 7); }

#define MFMA16(a, b, c) __builtin_amdgcn_mfma_f32_16x16x32_bf16((a), (b), (c), 0, 0, 0)

constexpr int K1_P = 6;

__global__ __launch_bounds__(256, 1) void k_qattn(const unsigned short* __restrict__ x,
                                                   const unsigned short* __restrict__ kc,
                                                   const unsigned short* __restrict__ vc,
                                                   const unsigned short* __restrict__ wq,
                                                   unsigned short* __restrict__ o) {
  __shared__ __attribute__((aligned(16))) char smem[2 * 16384 + 16384];
  char* const s_w = smem;
  char* const s_v = smem + 2 * 16384;

  const int bid = blockIdx.x;
  const int xcd = bid & 7, idx = bid >> 3;
  const int h = 4 * xcd + (idx & 3), b = idx >> 2;
  const int tid = threadIdx.x, lane = tid & 63, w = tid >> 6;
  const int l15 = lane & 15, g = lane >> 4;
  const int q4 = l15 >> 2, p4 = l15 & 3;

  const unsigned short* wq_h = wq + h * cDH;
  const unsigned short* x_row = x + (size_t)(b * cSQ + 16 * w + l15) * cD + 8 * g;
  const size_t kv_off = (size_t)(b * cH + h) * cSK * cDH;

  u32x4 wreg[K1_P][4];
  u32x4 xreg[K1_P][2];

  auto issue = [&](int s, int slot) {
#pragma unroll
    for (int i = 0; i < 4; ++i) {
      const int id = i * 256 + tid, row = id >> 4, c = id & 15;
      wreg[slot][i] = *(const u32x4*)(wq_h + (size_t)(64 * s + row) * cHD + c * 8);
    }
    xreg[slot][0] = *(const u32x4*)(x_row + 64 * s);
    xreg[slot][1] = *(const u32x4*)(x_row + 64 * s + 32);
  };

#pragma unroll
  for (int s = 0; s < K1_P; ++s) issue(s, s);

  u32x4 vreg[4];
#pragma unroll
  for (int i = 0; i < 4; ++i) {
    const int id = i * 256 + tid, row = id >> 4, c = id & 15;
    vreg[i] = *(const u32x4*)(vc + kv_off + row * cDH + c * 8);
  }
  u32x2 kfr[4][4][2];
#pragma unroll
  for (int kt = 0; kt < 4; ++kt)
#pragma unroll
    for (int s = 0; s < 4; ++s)
#pragma unroll
      for (int hf = 0; hf < 2; ++hf)
        kfr[kt][s][hf] = *(const u32x2*)(kc + kv_off + (16 * kt + l15) * cDH + 32 * s + 16 * hf + 4 * g);

  f32x4 acc[8];
#pragma unroll
  for (int t = 0; t < 8; ++t) acc[t] = (f32x4){0.f, 0.f, 0.f, 0.f};

  int woff[4];
#pragma unroll
  for (int i = 0; i < 4; ++i) {
    const int id = i * 256 + tid, row = id >> 4, c = id & 15;
    const int f = ((row & 3) | ((row >> 1) & 4)) << 1;
    woff[i] = row * 256 + ((c ^ f) << 4);
  }
  const int fl = (q4 | ((g & 1) << 2)) << 1;

#pragma unroll
  for (int s = 0; s < 16; ++s) {
    const int slot = s % K1_P;
    char* buf = s_w + (s & 1) * 16384;
#pragma unroll
    for (int i = 0; i < 4; ++i) *(u32x4*)(buf + woff[i]) = wreg[slot][i];
    const bf16x8 xb0 = __builtin_bit_cast(bf16x8, xreg[slot][0]);
    const bf16x8 xb1 = __builtin_bit_cast(bf16x8, xreg[slot][1]);
    if (s + K1_P < 16) issue(s + K1_P, slot);
    if (s == 8) {
#pragma unroll
      for (int i = 0; i < 4; ++i) {
        const int id = i * 256 + tid, row = id >> 4, c = id & 15;
        *(u32x4*)(s_v + row * 256 + ((c ^ ((row & 7) << 1)) << 4)) = vreg[i];
      }
    }
    __syncthreads();
#pragma unroll
    for (int kk = 0; kk < 2; ++kk) {
#pragma unroll
      for (int t = 0; t < 8; ++t) {
        const int c = 2 * t + (p4 >> 1);
        const int base = (32 * kk + 8 * g + q4) * 256 + ((c ^ fl) << 4) + 8 * (p4 & 1);
        const s16x4 a0 = tr_read(buf + base);
        const s16x4 a1 = tr_read(buf + base + 4 * 256);
        acc[t] = MFMA16(cat8(a0, a1), kk == 0 ? xb0 : xb1, acc[t]);
      }
    }
  }

  bf16x8 qf[4];
#pragma unroll
  for (int s = 0; s < 4; ++s) {
    u32x4 t;
    t[0] = pack_bf16(acc[2 * s][0], acc[2 * s][1]);
    t[1] = pack_bf16(acc[2 * s][2], acc[2 * s][3]);
    t[2] = pack_bf16(acc[2 * s + 1][0], acc[2 * s + 1][1]);
    t[3] = pack_bf16(acc[2 * s + 1][2], acc[2 * s + 1][3]);
    qf[s] = __builtin_bit_cast(bf16x8, t);
  }
  f32x4 sc[4];
#pragma unroll
  for (int kt = 0; kt < 4; ++kt) {
    sc[kt] = (f32x4){0.f, 0.f, 0.f, 0.f};
#pragma unroll
    for (int s = 0; s < 4; ++s) {
      u32x4 kf;
      kf[0] = kfr[kt][s][0][0]; kf[1] = kfr[kt][s][0][1];
      kf[2] = kfr[kt][s][1][0]; kf[3] = kfr[kt][s][1][1];
      sc[kt] = MFMA16(__builtin_bit_cast(bf16x8, kf), qf[s], sc[kt]);
    }
  }
  const float scale = 0.08838834764831845f;
  float mx = -3.0e38f;
#pragma unroll
  for (int kt = 0; kt < 4; ++kt)
#pragma unroll
    for (int r = 0; r < 4; ++r) { sc[kt][r] *= scale; mx = fmaxf(mx, sc[kt][r]); }
  mx = fmaxf(mx, __shfl_xor(mx, 16));
  mx = fmaxf(mx, __shfl_xor(mx, 32));
  float sum = 0.f;
#pragma unroll
  for (int kt = 0; kt < 4; ++kt)
#pragma unroll
    for (int r = 0; r < 4; ++r) { sc[kt][r] = __expf(sc[kt][r] - mx); sum += sc[kt][r]; }
  sum += __shfl_xor(sum, 16);
  sum += __shfl_xor(sum, 32);
  const float inv = 1.0f / sum;
  bf16x8 pf[2];
#pragma unroll
  for (int u = 0; u < 2; ++u) {
    u32x4 t;
    t[0] = pack_bf16(sc[2 * u][0] * inv, sc[2 * u][1] * inv);
    t[1] = pack_bf16(sc[2 * u][2] * inv, sc[2 * u][3] * inv);
    t[2] = pack_bf16(sc[2 * u + 1][0] * inv, sc[2 * u + 1][1] * inv);
    t[3] = pack_bf16(sc[2 * u + 1][2] * inv, sc[2 * u + 1][3] * inv);
    pf[u] = __builtin_bit_cast(bf16x8, t);
  }
  unsigned short* o_row = o + (size_t)(b * cSQ + 16 * w + l15) * cHD + h * cDH + 4 * g;
  const int fv = ((4 * g + q4) & 7) << 1;
#pragma unroll
  for (int t = 0; t < 8; ++t) {
    f32x4 oa = (f32x4){0.f, 0.f, 0.f, 0.f};
#pragma unroll
    for (int u = 0; u < 2; ++u) {
      const int c = 2 * t + (p4 >> 1);
      const int base = (32 * u + 4 * g + q4) * 256 + ((c ^ fv) << 4) + 8 * (p4 & 1);
      const s16x4 a0 = tr_read(s_v + base);
      const s16x4 a1 = tr_read(s_v + base + 16 * 256);
      oa = MFMA16(cat8(a0, a1), pf[u], oa);
    }
    u32x2 st;
    st[0] = pack_bf16(oa[0], oa[1]);
    st[1] = pack_bf16(oa[2], oa[3]);
    *(u32x2*)(o_row + 16 * t) = st;
  }
}

constexpr int K2_P = 4;

__global__ __launch_bounds__(256, 1) void k_oproj(const unsigned short* __restrict__ o,
                                                   const unsigned short* __restrict__ wo,
                                                   unsigned short* __restrict__ out) {
  __shared__ __attribute__((aligned(16))) char smem[4 * 8192 + 4 * 512];
  const int bid = blockIdx.x;
  const int xcd = bid & 7, idx = bid >> 3;
  const int n0 = 64 * (2 * xcd + (idx & 1)), m0 = 32 * (idx >> 1);
  const int tid = threadIdx.x, lane = tid & 63, w = tid >> 6;
  const int l15 = lane & 15, g = lane >> 4;
  const int q4 = l15 >> 2, p4 = l15 & 3;
  char* const s_b = smem + w * 8192;

  const unsigned short* wo_w = wo + (size_t)(1024 * w) * cD + n0;
  const unsigned short* o_w = o + (size_t)(m0 + l15) * cHD + 1024 * w + 8 * g;

  u32x4 wreg[K2_P][8];
  u32x4 oreg[K2_P][4];
  auto issue = [&](int s, int slot) {
#pragma unroll
    for (int i = 0; i < 8; ++i) {
      const int row = 8 * i + (lane >> 3), c = lane & 7;
      wreg[slot][i] = *(const u32x4*)(wo_w + (size_t)(64 * s + row) * cD + c * 8);
    }
#pragma unroll
    for (int kk = 0; kk < 2; ++kk)
#pragma unroll
      for (int mt = 0; mt < 2; ++mt)
        oreg[slot][2 * kk + mt] = *(const u32x4*)(o_w + (size_t)(16 * mt) * cHD + 64 * s + 32 * kk);
  };
#pragma unroll
  for (int s = 0; s < K2_P; ++s) issue(s, s);

  f32x4 acc[4][2];
#pragma unroll
  for (int nt = 0; nt < 4; ++nt)
#pragma unroll
    for (int mt = 0; mt < 2; ++mt) acc[nt][mt] = (f32x4){0.f, 0.f, 0.f, 0.f};

  const int wrow = lane >> 3, wc = lane & 7;
  const int wf = (((wrow >> 1) & 1)) << 1;
  const int fl = (((q4 >> 1) & 1) | ((g & 1) << 1)) << 1;

#pragma unroll
  for (int s = 0; s < 16; ++s) {
    const int slot = s % K2_P;
#pragma unroll
    for (int i = 0; i < 8; ++i) {
      const int f = wf | ((i & 1) << 2);
      *(u32x4*)(s_b + (8 * i + wrow) * 128 + ((wc ^ f) << 4)) = wreg[slot][i];
    }
    bf16x8 ob[4];
#pragma unroll
    for (int j = 0; j < 4; ++j) ob[j] = __builtin_bit_cast(bf16x8, oreg[slot][j]);
    if (s + K2_P < 16) issue(s + K2_P, slot);
#pragma unroll
    for (int kk = 0; kk < 2; ++kk) {
#pragma unroll
      for (int nt = 0; nt < 4; ++nt) {
        const int c = 2 * nt + (p4 >> 1);
        const int base = (32 * kk + 8 * g + q4) * 128 + ((c ^ fl) << 4) + 8 * (p4 & 1);
        const s16x4 a0 = tr_read(s_b + base);
        const s16x4 a1 = tr_read(s_b + base + 4 * 128);
        const bf16x8 af = cat8(a0, a1);
#pragma unroll
        for (int mt = 0; mt < 2; ++mt) acc[nt][mt] = MFMA16(af, ob[2 * kk + mt], acc[nt][mt]);
      }
    }
  }
  __syncthreads();
  char* s_r = smem + w * (32 * 272);
#pragma unroll
  for (int nt = 0; nt < 4; ++nt)
#pragma unroll
    for (int mt = 0; mt < 2; ++mt)
      *(f32x4*)(s_r + (16 * mt + l15) * 272 + (16 * nt + 4 * g) * 4) = acc[nt][mt];
  __syncthreads();
  {
    const int m = tid >> 3, nb = (tid & 7) * 8;
    f32x4 s0 = (f32x4){0.f, 0.f, 0.f, 0.f}, s1 = s0;
#pragma unroll
    for (int ww = 0; ww < 4; ++ww) {
      const char* p = smem + ww * (32 * 272) + m * 272 + nb * 4;
      s0 += *(const f32x4*)p;
      s1 += *(const f32x4*)(p + 16);
    }
    u32x4 st;
    st[0] = pack_bf16(s0[0], s0[1]); st[1] = pack_bf16(s0[2], s0[3]);
    st[2] = pack_bf16(s1[0], s1[1]); st[3] = pack_bf16(s1[2], s1[3]);
    *(u32x4*)(out + (size_t)(m0 + m) * cD + n0 + nb) = st;
  }
}

extern "C" void kernel_launch(void* const* d_in, const int* in_sizes, int n_in,
                              void* d_out, int out_size, void* d_ws, size_t ws_size,
                              hipStream_t stream) {
  const unsigned short* x = (const unsigned short*)d_in[0];
  const unsigned short* kc = (const unsigned short*)d_in[1];
  const unsigned short* vc = (const unsigned short*)d_in[2];
  const unsigned short* wq = (const unsigned short*)d_in[3];
  const unsigned short* wo = (const unsigned short*)d_in[4];
  unsigned short* out = (unsigned short*)d_out;
  unsigned short* o = (unsigned short*)d_ws;
  k_qattn<<<256, 256, 0, stream>>>(x, kc, vc, wq, o);
  k_oproj<<<256, 256, 0, stream>>>(o, wo, out);
}
```

```cpp
#include <hip/hip_runtime.h>
#include <hip/hip_bf16.h>
#include <stdint.h>

typedef __attribute__((ext_vector_type(8))) short bf16x8;
typedef __attribute__((ext_vector_type(4))) short s16x4;
typedef __attribute__((ext_vector_type(4))) float f32x4;
typedef __attribute__((ext_vector_type(4))) unsigned u32x4;
typedef __attribute__((ext_vector_type(2))) unsigned u32x2;
typedef __attribute__((address_space(3))) s16x4* lds_tr_ptr;
typedef __attribute__((address_space(3))) void* lds_vptr;
typedef __attribute__((address_space(1))) void* glb_vptr;

#define DI __device__ __forceinline__

constexpr int cB = 8, cSQ = 64, cD = 1024, cH = 32, cSK = 64, cDH = 128;
constexpr int cHD = cH * cDH;

DI unsigned pack_bf16(float lo, float hi) {
  __hip_bfloat16 a = __float2bfloat16(lo), b = __float2bfloat16(hi);
  unsigned short ua = __builtin_bit_cast(unsigned short, a), ub = __builtin_bit_cast(unsigned short, b);
  return (unsigned)ua | ((unsigned)ub << 16);
}

DI s16x4 tr_read(const char* p) { return __builtin_amdgcn_ds_read_tr16_b64_v4i16((lds_tr_ptr)(p)); }
DI bf16x8 cat8(s16x4 lo, s16x4 hi) { return __builtin_shufflevector(lo, hi, 0, 1, 2, 3, 4, 5, 6, 7); }

DI void glds16(const void* g, char* lds_base) {
  const unsigned dst = (unsigned)(size_t)lds_base;
  unsigned keep;
  asm volatile("s_mov_b32 %0, m0\n\ts_mov_b32 m0, %2\n\ts_nop 0\n\tglobal_load_lds_dwordx4 %1, off\n\ts_mov_b32 m0, %0"
               : "=&s"(keep) : "v"(g), "s"(dst) : "memory");
}

#define MFMA16(a, b, c) __builtin_amdgcn_mfma_f32_16x16x32_bf16((a), (b), (c), 0, 0, 0)
#define WAIT_VM(n) asm volatile("s_waitcnt vmcnt(" #n ")" ::: "memory")
#define WAIT_LGKM0() asm volatile("s_waitcnt lgkmcnt(0)" ::: "memory")
#define SCHED() __builtin_amdgcn_sched_barrier(0)

template <int N> DI void wait_vm() {
  static_assert(N >= 0 && N < 64, "vmcnt range");
  asm volatile("s_waitcnt vmcnt(%0)" ::"n"(N) : "memory");
}

constexpr int K1_NS = 6;
constexpr int K1_SLOT = 24576;

template <int S> DI void k1_wait_stage() {
}

__global__ __launch_bounds__(256, 1) void k_qattn(const unsigned short* __restrict__ x,
                                                   const unsigned short* __restrict__ kc,
                                                   const unsigned short* __restrict__ vc,
                                                   const unsigned short* __restrict__ wq,
                                                   unsigned short* __restrict__ o) {
  __shared__ __attribute__((aligned(16))) char smem[K1_NS * K1_SLOT];

  const int bid = blockIdx.x;
  const int xcd = bid & 7, idx = bid >> 3;
  const int h = 4 * xcd + (idx & 3), b = idx >> 2;
  const int tid = threadIdx.x, lane = tid & 63;
  const int w = __builtin_amdgcn_readfirstlane(tid >> 6);
  const int l15 = lane & 15, g = lane >> 4;
  const int q4 = l15 >> 2, p4 = l15 & 3;

  const unsigned short* wq_src[4];
#pragma unroll
  for (int i = 0; i < 4; ++i) {
    const int r = 16 * i + 4 * w + g;
    const int f = ((r & 3) | ((r >> 1) & 4)) << 1;
    wq_src[i] = wq + (size_t)r * cHD + h * cDH + ((l15 ^ f) * 8);
  }
  const unsigned short* x_src[2];
#pragma unroll
  for (int j = 0; j < 2; ++j) {
    const int r = 16 * w + 8 * j + (lane >> 3);
    x_src[j] = x + (size_t)(b * cSQ + r) * cD + (((lane & 7) ^ ((r >> 1) & 7)) * 8);
  }
  const size_t kv_off = (size_t)(b * cH + h) * cSK * cDH;

  auto issue_stage = [&](int s) {
    char* slot = smem + (s % K1_NS) * K1_SLOT;
#pragma unroll
    for (int i = 0; i < 4; ++i) glds16(wq_src[i] + (size_t)(64 * s) * cHD, slot + (16 * i + 4 * w) * 256);
#pragma unroll
    for (int j = 0; j < 2; ++j) glds16(x_src[j] + 64 * s, slot + 16384 + (16 * w + 8 * j) * 128);
  };
  auto issue_k = [&]() {
    char* slot = smem + (16 % K1_NS) * K1_SLOT;
#pragma unroll
    for (int i = 0; i < 4; ++i) {
      const int r = 16 * i + 4 * w + g;
      glds16(kc + kv_off + r * cDH + ((l15 ^ (r & 15)) * 8), slot + (16 * i + 4 * w) * 256);
    }
  };
  auto issue_v = [&]() {
    char* slot = smem + (17 % K1_NS) * K1_SLOT;
#pragma unroll
    for (int i = 0; i < 4; ++i) {
      const int r = 16 * i + 4 * w + g;
      glds16(vc + kv_off + r * cDH + ((l15 ^ ((r & 7) << 1)) * 8), slot + (16 * i + 4 * w) * 256);
    }
  };

  const int fl = (q4 | ((g & 1) << 2)) << 1;
  int roff[8];
#pragma unroll
  for (int t = 0; t < 8; ++t) roff[t] = (8 * g + q4) * 256 + (((2 * t + (p4 >> 1)) ^ fl) << 4) + 8 * (p4 & 1);
  const int xrow = 16 * w + l15;
  const int xoff0 = 16384 + xrow * 128 + (((0 + g) ^ ((xrow >> 1) & 7)) << 4);
  const int xoff1 = 16384 + xrow * 128 + (((4 + g) ^ ((xrow >> 1) & 7)) << 4);

  f32x4 acc[8];
#pragma unroll
  for (int t = 0; t < 8; ++t) acc[t] = (f32x4){0.f, 0.f, 0.f, 0.f};

  bf16x8 fa[8], fb[8];
  auto read_frags = [&](int s, int kk, bf16x8* f) {
    const char* buf = smem + (s % K1_NS) * K1_SLOT + kk * 32 * 256;
#pragma unroll
    for (int t = 0; t < 8; ++t) f[t] = cat8(tr_read(buf + roff[t]), tr_read(buf + roff[t] + 4 * 256));
  };
  auto read_x = [&](int s, bf16x8& x0, bf16x8& x1) {
    const char* buf = smem + (s % K1_NS) * K1_SLOT;
    x0 = *(const bf16x8*)(buf + xoff0);
    x1 = *(const bf16x8*)(buf + xoff1);
  };

#pragma unroll
  for (int s = 0; s < 5; ++s) issue_stage(s);
  wait_vm<24>();
  __builtin_amdgcn_s_barrier();
  asm volatile("" ::: "memory");
  bf16x8 xb0, xb1;
  read_x(0, xb0, xb1);
  read_frags(0, 0, fa);

#pragma unroll
  for (int s = 0; s < 16; ++s) {
    read_frags(s, 1, fb);
    SCHED();
#pragma unroll
    for (int t = 0; t < 8; ++t) acc[t] = MFMA16(fa[t], xb0, acc[t]);
    SCHED();
    bf16x8 nx0 = xb0, nx1 = xb1;
    if (s + 1 < 16) {
      if (s <= 10) wait_vm<18>();
      else if (s == 11) wait_vm<18>();
      else if (s == 12) wait_vm<16>();
      else if (s == 13) wait_vm<14>();
      else wait_vm<8>();
    }
    WAIT_LGKM0();
    __builtin_amdgcn_s_barrier();
    asm volatile("" ::: "memory");
    if (s + 5 < 16) issue_stage(s + 5);
    else if (s == 11) issue_k();
    else if (s == 12) issue_v();
    if (s + 1 < 16) {
      read_x(s + 1, nx0, nx1);
      read_frags(s + 1, 0, fa);
    }
    SCHED();
#pragma unroll
    for (int t = 0; t < 8; ++t) acc[t] = MFMA16(fb[t], xb1, acc[t]);
    SCHED();
    xb0 = nx0; xb1 = nx1;
  }
  wait_vm<0>();
  __builtin_amdgcn_s_barrier();
  asm volatile("" ::: "memory");
  const char* s_k = smem + (16 % K1_NS) * K1_SLOT;
  const char* s_v = smem + (17 % K1_NS) * K1_SLOT;

  bf16x8 qf[4];
#pragma unroll
  for (int s = 0; s < 4; ++s) {
    u32x4 t;
    t[0] = pack_bf16(acc[2 * s][0], acc[2 * s][1]);
    t[1] = pack_bf16(acc[2 * s][2], acc[2 * s][3]);
    t[2] = pack_bf16(acc[2 * s + 1][0], acc[2 * s + 1][1]);
    t[3] = pack_bf16(acc[2 * s + 1][2], acc[2 * s + 1][3]);
    qf[s] = __builtin_bit_cast(bf16x8, t);
  }
  f32x4 sc[4];
#pragma unroll
  for (int kt = 0; kt < 4; ++kt) {
    sc[kt] = (f32x4){0.f, 0.f, 0.f, 0.f};
    const int key = 16 * kt + l15;
#pragma unroll
    for (int s = 0; s < 4; ++s) {
      const u32x2 k0 = *(const u32x2*)(s_k + key * 256 + (((4 * s + (g >> 1)) ^ (key & 15)) << 4) + 8 * (g & 1));
      const u32x2 k1 = *(const u32x2*)(s_k + key * 256 + (((4 * s + 2 + (g >> 1)) ^ (key & 15)) << 4) + 8 * (g & 1));
      u32x4 kf;
      kf[0] = k0[0]; kf[1] = k0[1]; kf[2] = k1[0]; kf[3] = k1[1];
      sc[kt] = MFMA16(__builtin_bit_cast(bf16x8, kf), qf[s], sc[kt]);
    }
  }
  const float scale = 0.08838834764831845f;
  float mx = -3.0e38f;
#pragma unroll
  for (int kt = 0; kt < 4; ++kt)
#pragma unroll
    for (int r = 0; r < 4; ++r) { sc[kt][r] *= scale; mx = fmaxf(mx, sc[kt][r]); }
  mx = fmaxf(mx, __shfl_xor(mx, 16));
  mx = fmaxf(mx, __shfl_xor(mx, 32));
  float sum = 0.f;
#pragma unroll
  for (int kt = 0; kt < 4; ++kt)
#pragma unroll
    for (int r = 0; r < 4; ++r) { sc[kt][r] = __expf(sc[kt][r] - mx); sum += sc[kt][r]; }
  sum += __shfl_xor(sum, 16);
  sum += __shfl_xor(sum, 32);
  const float inv = 1.0f / sum;
  bf16x8 pf[2];
#pragma unroll
  for (int u = 0; u < 2; ++u) {
    u32x4 t;
    t[0] = pack_bf16(sc[2 * u][0] * inv, sc[2 * u][1] * inv);
    t[1] = pack_bf16(sc[2 * u][2] * inv, sc[2 * u][3] * inv);
    t[2] = pack_bf16(sc[2 * u + 1][0] * inv, sc[2 * u + 1][1] * inv);
    t[3] = pack_bf16(sc[2 * u + 1][2] * inv, sc[2 * u + 1][3] * inv);
    pf[u] = __builtin_bit_cast(bf16x8, t);
  }
  unsigned short* o_row = o + (size_t)(b * cSQ + 16 * w + l15) * cHD + h * cDH + 4 * g;
  const int fv = ((4 * g + q4) & 7) << 1;
#pragma unroll
  for (int t = 0; t < 8; ++t) {
    f32x4 oa = (f32x4){0.f, 0.f, 0.f, 0.f};
#pragma unroll
    for (int u = 0; u < 2; ++u) {
      const int c = 2 * t + (p4 >> 1);
      const int base = (32 * u + 4 * g + q4) * 256 + ((c ^ fv) << 4) + 8 * (p4 & 1);
      const s16x4 a0 = tr_read(s_v + base);
      const s16x4 a1 = tr_read(s_v + base + 16 * 256);
      oa = MFMA16(cat8(a0, a1), pf[u], oa);
    }
    u32x2 st;
    st[0] = pack_bf16(oa[0], oa[1]);
    st[1] = pack_bf16(oa[2], oa[3]);
    *(u32x2*)(o_row + 16 * t) = st;
  }
}

constexpr int K2_NS = 3;
constexpr int K2_SLOT = 12288;

__global__ __launch_bounds__(256, 1) void k_oproj(const unsigned short* __restrict__ o,
                                                   const unsigned short* __restrict__ wo,
                                                   unsigned short* __restrict__ out) {
  __shared__ __attribute__((aligned(16))) char smem[4 * K2_NS * K2_SLOT];
  const int bid = blockIdx.x;
  const int xcd = bid & 7, idx = bid >> 3;
  const int n0 = 64 * (2 * xcd + (idx & 1)), m0 = 32 * (idx >> 1);
  const int tid = threadIdx.x, lane = tid & 63;
  const int w = __builtin_amdgcn_readfirstlane(tid >> 6);
  const int l15 = lane & 15, g = lane >> 4;
  const int q4 = l15 >> 2, p4 = l15 & 3;
  char* const ring = smem + w * (K2_NS * K2_SLOT);

  const int prow = lane >> 3, pc = lane & 7;
  const unsigned short* wo_src0 = wo + (size_t)(1024 * w + prow) * cD + n0 + ((pc ^ (((prow >> 1) & 1) << 1)) * 8);
  const unsigned short* wo_src1 = wo + (size_t)(1024 * w + prow) * cD + n0 + ((pc ^ ((((prow >> 1) & 1) | 2) << 1)) * 8);
  const unsigned short* o_src[4];
#pragma unroll
  for (int j = 0; j < 4; ++j) {
    const int r = 8 * j + prow;
    o_src[j] = o + (size_t)(m0 + r) * cHD + 1024 * w + ((pc ^ ((r >> 1) & 7)) * 8);
  }
  auto issue_stage = [&](int s) {
    char* slot = ring + (s % K2_NS) * K2_SLOT;
#pragma unroll
    for (int i = 0; i < 8; ++i)
      glds16(((i & 1) ? wo_src1 : wo_src0) + (size_t)(64 * s + 8 * i) * cD, slot + i * 1024);
#pragma unroll
    for (int j = 0; j < 4; ++j) glds16(o_src[j] + 64 * s, slot + 8192 + j * 1024);
  };

  const int fl = (((q4 >> 1) & 1) | ((g & 1) << 1)) << 1;
  int roff[4];
#pragma unroll
  for (int nt = 0; nt < 4; ++nt) roff[nt] = (8 * g + q4) * 128 + (((2 * nt + (p4 >> 1)) ^ fl) << 4) + 8 * (p4 & 1);
  int boff[2][2];
#pragma unroll
  for (int kk = 0; kk < 2; ++kk)
#pragma unroll
    for (int mt = 0; mt < 2; ++mt) {
      const int r = 16 * mt + l15;
      boff[kk][mt] = 8192 + r * 128 + (((4 * kk + g) ^ ((r >> 1) & 7)) << 4);
    }

  f32x4 acc[4][2];
#pragma unroll
  for (int nt = 0; nt < 4; ++nt)
#pragma unroll
    for (int mt = 0; mt < 2; ++mt) acc[nt][mt] = (f32x4){0.f, 0.f, 0.f, 0.f};

  issue_stage(0);
  issue_stage(1);
#pragma unroll
  for (int s = 0; s < 16; ++s) {
    if (s + 1 < 16) wait_vm<12>(); else wait_vm<0>();
    if (s + 2 < 16) issue_stage(s + 2);
    const char* buf = ring + (s % K2_NS) * K2_SLOT;
    bf16x8 fa[2][4], fb[2][2];
#pragma unroll
    for (int kk = 0; kk < 2; ++kk) {
#pragma unroll
      for (int nt = 0; nt < 4; ++nt)
        fa[kk][nt] = cat8(tr_read(buf + kk * 32 * 128 + roff[nt]), tr_read(buf + kk * 32 * 128 + roff[nt] + 4 * 128));
#pragma unroll
      for (int mt = 0; mt < 2; ++mt) fb[kk][mt] = *(const bf16x8*)(buf + boff[kk][mt]);
    }
#pragma unroll
    for (int kk = 0; kk < 2; ++kk)
#pragma unroll
      for (int nt = 0; nt < 4; ++nt)
#pragma unroll
        for (int mt = 0; mt < 2; ++mt) acc[nt][mt] = MFMA16(fa[kk][nt], fb[kk][mt], acc[nt][mt]);
    WAIT_LGKM0();
  }
  __syncthreads();
  char* s_r = smem + w * (32 * 272);
#pragma unroll
  for (int nt = 0; nt < 4; ++nt)
#pragma unroll
    for (int mt = 0; mt < 2; ++mt)
      *(f32x4*)(s_r + (16 * mt + l15) * 272 + (16 * nt + 4 * g) * 4) = acc[nt][mt];
  __syncthreads();
  {
    const int m = tid >> 3, nb = (tid & 7) * 8;
    f32x4 s0 = (f32x4){0.f, 0.f, 0.f, 0.f}, s1 = s0;
#pragma unroll
    for (int ww = 0; ww < 4; ++ww) {
      const char* p = smem + ww * (32 * 272) + m * 272 + nb * 4;
      s0 += *(const f32x4*)p;
      s1 += *(const f32x4*)(p + 16);
    }
    u32x4 st;
    st[0] = pack_bf16(s0[0], s0[1]); st[1] = pack_bf16(s0[2], s0[3]);
    st[2] = pack_bf16(s1[0], s1[1]); st[3] = pack_bf16(s1[2], s1[3]);
    *(u32x4*)(out + (size_t)(m0 + m) * cD + n0 + nb) = st;
  }
}

extern "C" void kernel_launch(void* const* d_in, const int* in_sizes, int n_in,
                              void* d_out, int out_size, void* d_ws, size_t ws_size,
                              hipStream_t stream) {
  const unsigned short* x = (const unsigned short*)d_in[0];
  const unsigned short* kc = (const unsigned short*)d_in[1];
  const unsigned short* vc = (const unsigned short*)d_in[2];
  const unsigned short* wq = (const unsigned short*)d_in[3];
  const unsigned short* wo = (const unsigned short*)d_in[4];
  unsigned short* out = (unsigned short*)d_out;
  unsigned short* o = (unsigned short*)d_ws;
  k_qattn<<<256, 256, 0, stream>>>(x, kc, vc, wq, o);
  k_oproj<<<256, 256, 0, stream>>>(o, wo, out);
}
```

```cpp
#include <hip/hip_runtime.h>
#include <hip/hip_bf16.h>
#include <stdint.h>

typedef __attribute__((ext_vector_type(8))) short bf16x8;
typedef __attribute__((ext_vector_type(4))) short s16x4;
typedef __attribute__((ext_vector_type(4))) float f32x4;
typedef __attribute__((ext_vector_type(16))) float f32x16;
typedef __attribute__((ext_vector_type(4))) unsigned u32x4;
typedef __attribute__((ext_vector_type(2))) unsigned u32x2;
typedef __attribute__((address_space(3))) s16x4* lds_tr_ptr;
typedef __attribute__((address_space(3))) void* lds_vptr;
typedef __attribute__((address_space(1))) void* glb_vptr;

#define DI __device__ __forceinline__

constexpr int cB = 8, cSQ = 64, cD = 1024, cH = 32, cSK = 64, cDH = 128;
constexpr int cHD = cH * cDH;

DI unsigned pack_bf16(float lo, float hi) {
  __hip_bfloat16 a = __float2bfloat16(lo), b = __float2bfloat16(hi);
  unsigned short ua = __builtin_bit_cast(unsigned short, a), ub = __builtin_bit_cast(unsigned short, b);
  return (unsigned)ua | ((unsigned)ub << 16);
}

DI s16x4 tr_read(const char* p) { return __builtin_amdgcn_ds_read_tr16_b64_v4i16((lds_tr_ptr)(p)); }
DI bf16x8 cat8(s16x4 lo, s16x4 hi) { return __builtin_shufflevector(lo, hi, 0, 1, 2, 3, 4, 5, 6, 7); }

DI void glds16(const void* g, char* lds_base) {
  const unsigned dst = (unsigned)(size_t)lds_base;
  unsigned keep;
  asm volatile("s_mov_b32 %0, m0\n\ts_mov_b32 m0, %2\n\ts_nop 0\n\tglobal_load_lds_dwordx4 %1, off\n\ts_mov_b32 m0, %0"
               : "=&s"(keep) : "v"(g), "s"(dst) : "memory");
}

#define MFMA16(a, b, c) __builtin_amdgcn_mfma_f32_16x16x32_bf16((a), (b), (c), 0, 0, 0)
#define MFMA32(a, b, c) __builtin_amdgcn_mfma_f32_32x32x16_bf16((a), (b), (c), 0, 0, 0)
#define WAIT_VM(n) asm volatile("s_waitcnt vmcnt(" #n ")" ::: "memory")
#define WAIT_LGKM0() asm volatile("s_waitcnt lgkmcnt(0)" ::: "memory")
#define SCHED() __builtin_amdgcn_sched_barrier(0)

template <int N> DI void wait_vm() {
  static_assert(N >= 0 && N < 64, "vmcnt range");
  asm volatile("s_waitcnt vmcnt(%0)" ::"n"(N) : "memory");
}

constexpr int K1_NS = 6;
constexpr int K1_SLOT = 24576;

__global__ __launch_bounds__(256, 1) void k_qattn(const unsigned short* __restrict__ x,
                                                   const unsigned short* __restrict__ kc,
                                                   const unsigned short* __restrict__ vc,
                                                   const unsigned short* __restrict__ wq,
                                                   unsigned short* __restrict__ o) {
  __shared__ __attribute__((aligned(16))) char smem[K1_NS * K1_SLOT];

  const int bid = blockIdx.x;
  const int xcd = bid & 7, idx = bid >> 3;
  const int h = 4 * xcd + (idx & 3), b = idx >> 2;
  const int tid = threadIdx.x, lane = tid & 63;
  const int w = __builtin_amdgcn_readfirstlane(tid >> 6);
  const int dh = w >> 1, mh = w & 1;
  const int l15 = lane & 15, g = lane >> 4, l31 = lane & 31, hh = lane >> 5;
  const int q4 = l15 >> 2, p4 = l15 & 3, dblk = g & 1;

  const unsigned short* wq_src[4];
#pragma unroll
  for (int i = 0; i < 4; ++i) {
    const int r = 16 * i + 4 * w + g;
    wq_src[i] = wq + (size_t)r * cHD + h * cDH + ((l15 ^ ((r & 3) << 2)) * 8);
  }
  const unsigned short* x_src[2];
#pragma unroll
  for (int j = 0; j < 2; ++j) {
    const int r = 16 * w + 8 * j + (lane >> 3);
    x_src[j] = x + (size_t)(b * cSQ + r) * cD + (((lane & 7) ^ ((r >> 1) & 7)) * 8);
  }
  const size_t kv_off = (size_t)(b * cH + h) * cSK * cDH;

  auto issue_stage = [&](int s) {
    char* slot = smem + (s % K1_NS) * K1_SLOT;
#pragma unroll
    for (int i = 0; i < 4; ++i) glds16(wq_src[i] + (size_t)(64 * s) * cHD, slot + (16 * i + 4 * w) * 256);
#pragma unroll
    for (int j = 0; j < 2; ++j) glds16(x_src[j] + 64 * s, slot + 16384 + (16 * w + 8 * j) * 128);
  };
  auto issue_k = [&]() {
    char* slot = smem + (16 % K1_NS) * K1_SLOT;
#pragma unroll
    for (int i = 0; i < 4; ++i) {
      const int r = 16 * i + 4 * w + g;
      glds16(kc + kv_off + r * cDH + ((l15 ^ (r & 15)) * 8), slot + (16 * i + 4 * w) * 256);
    }
  };
  auto issue_v = [&]() {
    char* slot = smem + (17 % K1_NS) * K1_SLOT;
#pragma unroll
    for (int i = 0; i < 4; ++i) {
      const int r = 16 * i + 4 * w + g;
      glds16(vc + kv_off + r * cDH + ((l15 ^ ((r & 3) << 2)) * 8), slot + (16 * i + 4 * w) * 256);
    }
  };

  int aoff[2];
#pragma unroll
  for (int T = 0; T < 2; ++T)
    aoff[T] = (8 * hh + q4) * 256 + (((8 * dh + 4 * T + 2 * dblk + (p4 >> 1)) ^ (q4 << 2)) << 4) + 8 * (p4 & 1);
  const int xrow = 32 * mh + l31;
  const int xsw = (xrow >> 1) & 7;
  int boff[2];
#pragma unroll
  for (int k2 = 0; k2 < 2; ++k2) boff[k2] = 16384 + xrow * 128 + (((2 * k2 + hh) ^ xsw) << 4);

  f32x16 acc[2];
#pragma unroll
  for (int T = 0; T < 2; ++T)
#pragma unroll
    for (int i = 0; i < 16; ++i) acc[T][i] = 0.f;

  struct Frag { bf16x8 a[2][2]; bf16x8 b[2]; };
  auto read_half = [&](int s, int hf, Frag& f) {
    const char* buf = smem + (s % K1_NS) * K1_SLOT;
#pragma unroll
    for (int k2 = 0; k2 < 2; ++k2) {
#pragma unroll
      for (int T = 0; T < 2; ++T)
        f.a[k2][T] = cat8(tr_read(buf + aoff[T] + (32 * hf + 16 * k2) * 256), tr_read(buf + aoff[T] + (32 * hf + 16 * k2 + 4) * 256));
      f.b[k2] = *(const bf16x8*)(buf + (boff[k2] ^ (hf << 6)));
    }
  };
  auto mma_half = [&](const Frag& f) {
#pragma unroll
    for (int k2 = 0; k2 < 2; ++k2)
#pragma unroll
      for (int T = 0; T < 2; ++T) acc[T] = MFMA32(f.a[k2][T], f.b[k2], acc[T]);
  };

  Frag fa, fb;
#pragma unroll
  for (int s = 0; s < 5; ++s) issue_stage(s);
  wait_vm<24>();
  __builtin_amdgcn_s_barrier();
  asm volatile("" ::: "memory");
  read_half(0, 0, fa);

#pragma unroll
  for (int s = 0; s < 16; ++s) {
    read_half(s, 1, fb);
    SCHED();
    mma_half(fa);
    SCHED();
    if (s + 1 < 16) {
      if (s <= 11) wait_vm<18>();
      else if (s == 12) wait_vm<16>();
      else if (s == 13) wait_vm<14>();
      else wait_vm<8>();
    }
    WAIT_LGKM0();
    __builtin_amdgcn_s_barrier();
    asm volatile("" ::: "memory");
    if (s + 5 < 16) issue_stage(s + 5);
    else if (s == 11) issue_k();
    else if (s == 12) issue_v();
    if (s + 1 < 16) read_half(s + 1, 0, fa);
    SCHED();
    mma_half(fb);
    SCHED();
  }
  wait_vm<0>();
  __builtin_amdgcn_s_barrier();
  asm volatile("" ::: "memory");
  const char* s_k = smem + (16 % K1_NS) * K1_SLOT;
  const char* s_v = smem + (17 % K1_NS) * K1_SLOT;
  char* s_x = smem;

  bf16x8 qf[2][2];
#pragma unroll
  for (int T = 0; T < 2; ++T)
#pragma unroll
    for (int s2 = 0; s2 < 2; ++s2) {
      u32x4 t;
#pragma unroll
      for (int i = 0; i < 4; ++i) t[i] = pack_bf16(acc[T][8 * s2 + 2 * i], acc[T][8 * s2 + 2 * i + 1]);
      qf[T][s2] = __builtin_bit_cast(bf16x8, t);
    }
  f32x16 sc[2];
#pragma unroll
  for (int kt = 0; kt < 2; ++kt) {
#pragma unroll
    for (int i = 0; i < 16; ++i) sc[kt][i] = 0.f;
    const int key = 32 * kt + l31;
#pragma unroll
    for (int T = 0; T < 2; ++T)
#pragma unroll
      for (int s2 = 0; s2 < 2; ++s2) {
        const int d0 = 64 * dh + 32 * T + 16 * s2 + 4 * hh;
        const u32x2 k0 = *(const u32x2*)(s_k + key * 256 + (((d0 >> 3) ^ (key & 15)) << 4) + (d0 & 7) * 2);
        const u32x2 k1 = *(const u32x2*)(s_k + key * 256 + ((((d0 + 8) >> 3) ^ (key & 15)) << 4) + (d0 & 7) * 2);
        u32x4 kf;
        kf[0] = k0[0]; kf[1] = k0[1]; kf[2] = k1[0]; kf[3] = k1[1];
        sc[kt] = MFMA32(__builtin_bit_cast(bf16x8, kf), qf[T][s2], sc[kt]);
      }
  }
  {
    char* mine = s_x + w * 8192 + lane * 16;
#pragma unroll
    for (int kt = 0; kt < 2; ++kt)
#pragma unroll
      for (int i = 0; i < 4; ++i)
        *(f32x4*)(mine + (kt * 4 + i) * 1024) = (f32x4){sc[kt][4 * i], sc[kt][4 * i + 1], sc[kt][4 * i + 2], sc[kt][4 * i + 3]};
    __syncthreads();
    const char* theirs = s_x + (w ^ 2) * 8192 + lane * 16;
#pragma unroll
    for (int kt = 0; kt < 2; ++kt)
#pragma unroll
      for (int i = 0; i < 4; ++i) {
        const f32x4 v = *(const f32x4*)(theirs + (kt * 4 + i) * 1024);
        sc[kt][4 * i] += v[0]; sc[kt][4 * i + 1] += v[1]; sc[kt][4 * i + 2] += v[2]; sc[kt][4 * i + 3] += v[3];
      }
  }
  const float scale = 0.08838834764831845f;
  float mx = -3.0e38f;
#pragma unroll
  for (int kt = 0; kt < 2; ++kt)
#pragma unroll
    for (int i = 0; i < 16; ++i) { sc[kt][i] *= scale; mx = fmaxf(mx, sc[kt][i]); }
  mx = fmaxf(mx, __shfl_xor(mx, 32));
  float sum = 0.f;
#pragma unroll
  for (int kt = 0; kt < 2; ++kt)
#pragma unroll
    for (int i = 0; i < 16; ++i) { sc[kt][i] = __expf(sc[kt][i] - mx); sum += sc[kt][i]; }
  sum += __shfl_xor(sum, 32);
  const float inv = 1.0f / sum;
  bf16x8 pf[2][2];
#pragma unroll
  for (int kt = 0; kt < 2; ++kt)
#pragma unroll
    for (int s2 = 0; s2 < 2; ++s2) {
      u32x4 t;
#pragma unroll
      for (int i = 0; i < 4; ++i) t[i] = pack_bf16(sc[kt][8 * s2 + 2 * i] * inv, sc[kt][8 * s2 + 2 * i + 1] * inv);
      pf[kt][s2] = __builtin_bit_cast(bf16x8, t);
    }
  unsigned short* o_row = o + (size_t)(b * cSQ + 32 * mh + l31) * cHD + h * cDH + 64 * dh + 4 * hh;
#pragma unroll
  for (int T = 0; T < 2; ++T) {
    f32x16 oa;
#pragma unroll
    for (int i = 0; i < 16; ++i) oa[i] = 0.f;
#pragma unroll
    for (int kt = 0; kt < 2; ++kt)
#pragma unroll
      for (int s2 = 0; s2 < 2; ++s2) {
        const int r0 = 32 * kt + 16 * s2 + 4 * hh + q4;
        const int base = r0 * 256 + (((8 * dh + 4 * T + 2 * dblk + (p4 >> 1)) ^ (q4 << 2)) << 4) + 8 * (p4 & 1);
        const s16x4 a0 = tr_read(s_v + base);
        const s16x4 a1 = tr_read(s_v + base + 8 * 256);
        oa = MFMA32(cat8(a0, a1), pf[kt][s2], oa);
      }
#pragma unroll
    for (int i = 0; i < 4; ++i) {
      u32x2 st;
      st[0] = pack_bf16(oa[4 * i], oa[4 * i + 1]);
      st[1] = pack_bf16(oa[4 * i + 2], oa[4 * i + 3]);
      *(u32x2*)(o_row + 32 * T + 8 * i) = st;
    }
  }
}

constexpr int K2_NS = 3;
constexpr int K2_SLOT = 12288;

__global__ __launch_bounds__(256, 1) void k_oproj(const unsigned short* __restrict__ o,
                                                   const unsigned short* __restrict__ wo,
                                                   unsigned short* __restrict__ out) {
  __shared__ __attribute__((aligned(16))) char smem[4 * K2_NS * K2_SLOT];
  const int bid = blockIdx.x;
  const int xcd = bid & 7, idx = bid >> 3;
  const int n0 = 64 * (2 * xcd + (idx & 1)), m0 = 32 * (idx >> 1);
  const int tid = threadIdx.x, lane = tid & 63;
  const int w = __builtin_amdgcn_readfirstlane(tid >> 6);
  const int l15 = lane & 15, g = lane >> 4;
  const int q4 = l15 >> 2, p4 = l15 & 3;
  char* const ring = smem + w * (K2_NS * K2_SLOT);

  const int prow = lane >> 3, pc = lane & 7;
  const unsigned short* wo_src0 = wo + (size_t)(1024 * w + prow) * cD + n0 + ((pc ^ (((prow >> 1) & 1) << 1)) * 8);
  const unsigned short* wo_src1 = wo + (size_t)(1024 * w + prow) * cD + n0 + ((pc ^ ((((prow >> 1) & 1) | 2) << 1)) * 8);
  const unsigned short* o_src[4];
#pragma unroll
  for (int j = 0; j < 4; ++j) {
    const int r = 8 * j + prow;
    o_src[j] = o + (size_t)(m0 + r) * cHD + 1024 * w + ((pc ^ ((r >> 1) & 7)) * 8);
  }
  auto issue_stage = [&](int s) {
    char* slot = ring + (s % K2_NS) * K2_SLOT;
#pragma unroll
    for (int i = 0; i < 8; ++i)
      glds16(((i & 1) ? wo_src1 : wo_src0) + (size_t)(64 * s + 8 * i) * cD, slot + i * 1024);
#pragma unroll
    for (int j = 0; j < 4; ++j) glds16(o_src[j] + 64 * s, slot + 8192 + j * 1024);
  };

  const int fl = (((q4 >> 1) & 1) | ((g & 1) << 1)) << 1;
  int roff[4];
#pragma unroll
  for (int nt = 0; nt < 4; ++nt) roff[nt] = (8 * g + q4) * 128 + (((2 * nt + (p4 >> 1)) ^ fl) << 4) + 8 * (p4 & 1);
  int boff[2][2];
#pragma unroll
  for (int kk = 0; kk < 2; ++kk)
#pragma unroll
    for (int mt = 0; mt < 2; ++mt) {
      const int r = 16 * mt + l15;
      boff[kk][mt] = 8192 + r * 128 + (((4 * kk + g) ^ ((r >> 1) & 7)) << 4);
    }

  f32x4 acc[4][2];
#pragma unroll
  for (int nt = 0; nt < 4; ++nt)
#pragma unroll
    for (int mt = 0; mt < 2; ++mt) acc[nt][mt] = (f32x4){0.f, 0.f, 0.f, 0.f};

  issue_stage(0);
  issue_stage(1);
#pragma unroll
  for (int s = 0; s < 16; ++s) {
    if (s + 1 < 16) wait_vm<12>(); else wait_vm<0>();
    if (s + 2 < 16) issue_stage(s + 2);
    const char* buf = ring + (s % K2_NS) * K2_SLOT;
    bf16x8 fa[2][4], fb[2][2];
#pragma unroll
    for (int kk = 0; kk < 2; ++kk) {
#pragma unroll
      for (int nt = 0; nt < 4; ++nt)
        fa[kk][nt] = cat8(tr_read(buf + kk * 32 * 128 + roff[nt]), tr_read(buf + kk * 32 * 128 + roff[nt] + 4 * 128));
#pragma unroll
      for (int mt = 0; mt < 2; ++mt) fb[kk][mt] = *(const bf16x8*)(buf + boff[kk][mt]);
    }
#pragma unroll
    for (int kk = 0; kk < 2; ++kk)
#pragma unroll
      for (int nt = 0; nt < 4; ++nt)
#pragma unroll
        for (int mt = 0; mt < 2; ++mt) acc[nt][mt] = MFMA16(fa[kk][nt], fb[kk][mt], acc[nt][mt]);
    WAIT_LGKM0();
  }
  __syncthreads();
  char* s_r = smem + w * (32 * 272);
#pragma unroll
  for (int nt = 0; nt < 4; ++nt)
#pragma unroll
    for (int mt = 0; mt < 2; ++mt)
      *(f32x4*)(s_r + (16 * mt + l15) * 272 + (16 * nt + 4 * g) * 4) = acc[nt][mt];
  __syncthreads();
  {
    const int m = tid >> 3, nb = (tid & 7) * 8;
    f32x4 s0 = (f32x4){0.f, 0.f, 0.f, 0.f}, s1 = s0;
#pragma unroll
    for (int ww = 0; ww < 4; ++ww) {
      const char* p = smem + ww * (32 * 272) + m * 272 + nb * 4;
      s0 += *(const f32x4*)p;
      s1 += *(const f32x4*)(p + 16);
    }
    u32x4 st;
    st[0] = pack_bf16(s0[0], s0[1]); st[1] = pack_bf16(s0[2], s0[3]);
    st[2] = pack_bf16(s1[0], s1[1]); st[3] = pack_bf16(s1[2], s1[3]);
    *(u32x4*)(out + (size_t)(m0 + m) * cD + n0 + nb) = st;
  }
}

extern "C" void kernel_launch(void* const* d_in, const int* in_sizes, int n_in,
                              void* d_out, int out_size, void* d_ws, size_t ws_size,
                              hipStream_t stream) {
  const unsigned short* x = (const unsigned short*)d_in[0];
  const unsigned short* kc = (const unsigned short*)d_in[1];
  const unsigned short* vc = (const unsigned short*)d_in[2];
  const unsigned short* wq = (const unsigned short*)d_in[3];
  const unsigned short* wo = (const unsigned short*)d_in[4];
  unsigned short* out = (unsigned short*)d_out;
  unsigned short* o = (unsigned short*)d_ws;
  k_qattn<<<256, 256, 0, stream>>>(x, kc, vc, wq, o);
  k_oproj<<<256, 256, 0, stream>>>(o, wo, out);
}
```

```cpp
#include <hip/hip_runtime.h>
#include <hip/hip_bf16.h>
#include <stdint.h>

typedef __attribute__((ext_vector_type(8))) short bf16x8;
typedef __attribute__((ext_vector_type(4))) short s16x4;
typedef __attribute__((ext_vector_type(4))) float f32x4;
typedef __attribute__((ext_vector_type(16))) float f32x16;
typedef __attribute__((ext_vector_type(4))) unsigned u32x4;
typedef __attribute__((ext_vector_type(2))) unsigned u32x2;
typedef __attribute__((address_space(3))) s16x4* lds_tr_ptr;
typedef __attribute__((address_space(3))) void* lds_vptr;
typedef __attribute__((address_space(1))) void* glb_vptr;

#define DI __device__ __forceinline__

constexpr int cB = 8, cSQ = 64, cD = 1024, cH = 32, cSK = 64, cDH = 128;
constexpr int cHD = cH * cDH;

DI unsigned pack_bf16(float lo, float hi) {
  __hip_bfloat16 a = __float2bfloat16(lo), b = __float2bfloat16(hi);
  unsigned short ua = __builtin_bit_cast(unsigned short, a), ub = __builtin_bit_cast(unsigned short, b);
  return (unsigned)ua | ((unsigned)ub << 16);
}

DI s16x4 tr_read(const char* p) { return __builtin_amdgcn_ds_read_tr16_b64_v4i16((lds_tr_ptr)(p)); }
DI bf16x8 cat8(s16x4 lo, s16x4 hi) { return __builtin_shufflevector(lo, hi, 0, 1, 2, 3, 4, 5, 6, 7); }

DI void glds16(const void* g, char* lds_base) {
  const unsigned dst = (unsigned)(size_t)lds_base;
  unsigned keep;
  asm volatile("s_mov_b32 %0, m0\n\ts_mov_b32 m0, %2\n\ts_nop 0\n\tglobal_load_lds_dwordx4 %1, off\n\ts_mov_b32 m0, %0"
               : "=&s"(keep) : "v"(g), "s"(dst) : "memory");
}

#define MFMA16(a, b, c) __builtin_amdgcn_mfma_f32_16x16x32_bf16((a), (b), (c), 0, 0, 0)
#define MFMA32(a, b, c) __builtin_amdgcn_mfma_f32_32x32x16_bf16((a), (b), (c), 0, 0, 0)
#define WAIT_VM(n) asm volatile("s_waitcnt vmcnt(" #n ")" ::: "memory")
#define WAIT_LGKM0() asm volatile("s_waitcnt lgkmcnt(0)" ::: "memory")
#define SCHED() __builtin_amdgcn_sched_barrier(0)
#define ILV() do { _Pragma("unroll") for (int q_ = 0; q_ < 4; ++q_) { __builtin_amdgcn_sched_group_barrier(0x008, 1, 0); __builtin_amdgcn_sched_group_barrier(0x100, 3, 0); } } while (0)
#define STAMP_DECL
#define STAMP(i)
#define STAMP_FLUSH(base, n)

template <int N> DI void wait_vm() {
  static_assert(N >= 0 && N < 64, "vmcnt range");
  asm volatile("s_waitcnt vmcnt(%0)" ::"n"(N) : "memory");
}

constexpr int K1_NS = 6;
constexpr int K1_SLOT = 24576;

__global__ __launch_bounds__(512, 2) void k_qattn(const unsigned short* __restrict__ x,
                                                   const unsigned short* __restrict__ kc,
                                                   const unsigned short* __restrict__ vc,
                                                   const unsigned short* __restrict__ wq,
                                                   const unsigned short* __restrict__ wo_pf,
                                                   unsigned short* __restrict__ o) {
  __shared__ __attribute__((aligned(16))) char smem[K1_NS * K1_SLOT];
  STAMP_DECL;
  STAMP(0);
  const int bid = blockIdx.x;
  const int xcd = bid & 7, idx = bid >> 3;
  const int h = 4 * xcd + (idx & 3), b = idx >> 2;
  const int tid = threadIdx.x, lane = tid & 63;
  const int w = __builtin_amdgcn_readfirstlane(tid >> 6);
  const int l15 = lane & 15, g = lane >> 4, l31 = lane & 31, hh = lane >> 5;
  const int q4 = l15 >> 2, p4 = l15 & 3, dblk = g & 1;
  const size_t kv_off = (size_t)(b * cH + h) * cSK * cDH;
  char* const s_k = smem + 5 * K1_SLOT;
  char* const s_v = smem + 0 * K1_SLOT;
  char* const s_x = smem + 1 * K1_SLOT;

  if (w >= 4) {
    const int lw = w - 4;
    const unsigned short* wq_src[4];
#pragma unroll
    for (int i = 0; i < 4; ++i) {
      const int r = 16 * i + 4 * lw + g;
      wq_src[i] = wq + (size_t)r * cHD + h * cDH + ((l15 ^ ((r & 3) << 2)) * 8);
    }
    const unsigned short* x_src[2];
#pragma unroll
    for (int j = 0; j < 2; ++j) {
      const int r = 16 * lw + 8 * j + (lane >> 3);
      x_src[j] = x + (size_t)(b * cSQ + r) * cD + (((lane & 7) ^ ((r >> 1) & 7)) * 8);
    }
    auto issue_stage = [&](int s) {
      char* slot = smem + (s % K1_NS) * K1_SLOT;
#pragma unroll
      for (int i = 0; i < 4; ++i) glds16(wq_src[i] + (size_t)(64 * s) * cHD, slot + (16 * i + 4 * lw) * 256);
#pragma unroll
      for (int j = 0; j < 2; ++j) glds16(x_src[j] + 64 * s, slot + 16384 + (16 * lw + 8 * j) * 128);
    };
#pragma unroll
    for (int s = 0; s < 5; ++s) issue_stage(s);
    u32x4 kreg[4], vreg[4];
#pragma unroll
    for (int i = 0; i < 4; ++i) {
      const int r = 16 * i + 4 * lw + g;
      kreg[i] = *(const u32x4*)(kc + kv_off + r * cDH + l15 * 8);
      vreg[i] = *(const u32x4*)(vc + kv_off + r * cDH + l15 * 8);
    }
    wait_vm<32>();
    __builtin_amdgcn_s_barrier();
#pragma unroll
    for (int s = 0; s < 16; ++s) {
      if (s <= 3) wait_vm<26>();
      else if (s <= 11) wait_vm<18>();
      else if (s == 12) wait_vm<12>();
      else if (s == 13) wait_vm<6>();
      else wait_vm<0>();
      WAIT_LGKM0();
      __builtin_amdgcn_s_barrier();
      asm volatile("" ::: "memory");
      if (s + 5 < 16) issue_stage(s + 5);
      if (s == 12) {
#pragma unroll
        for (int i = 0; i < 4; ++i) {
          const int r = 16 * i + 4 * lw + g;
          *(u32x4*)(s_k + r * 256 + ((l15 ^ (r & 15)) << 4)) = kreg[i];
        }
      }
      if (s == 13) {
#pragma unroll
        for (int i = 0; i < 4; ++i) {
          const int r = 16 * i + 4 * lw + g;
          *(u32x4*)(s_v + r * 256 + ((l15 ^ ((r & 3) << 2)) << 4)) = vreg[i];
        }
      }
    }
    {
      const unsigned short* pf = wo_pf + (size_t)(idx * 128 + lw * 32 + g) * cD + xcd * 128 + l15 * 8;
      u32x4 acc4 = {0u, 0u, 0u, 0u};
#pragma unroll
      for (int i = 0; i < 8; ++i) acc4 ^= *(const u32x4*)(pf + (size_t)(4 * i) * cD);
      asm volatile("" ::"v"(acc4[0]), "v"(acc4[1]), "v"(acc4[2]), "v"(acc4[3]));
    }
    __syncthreads();
    return;
  }

  const int dh = w >> 1, mh = w & 1;
  int aoff[2];
#pragma unroll
  for (int T = 0; T < 2; ++T)
    aoff[T] = (8 * hh + q4) * 256 + (((8 * dh + 4 * T + 2 * dblk + (p4 >> 1)) ^ (q4 << 2)) << 4) + 8 * (p4 & 1);
  const int xrow = 32 * mh + l31;
  const int xsw = (xrow >> 1) & 7;
  int boff[2];
#pragma unroll
  for (int k2 = 0; k2 < 2; ++k2) boff[k2] = 16384 + xrow * 128 + (((2 * k2 + hh) ^ xsw) << 4);

  f32x16 acc[2];
#pragma unroll
  for (int T = 0; T < 2; ++T)
#pragma unroll
    for (int i = 0; i < 16; ++i) acc[T][i] = 0.f;

  struct Frag { bf16x8 a[2][2]; bf16x8 b[2]; };
  auto read_half = [&](int s, int hf, Frag& f) {
    const char* buf = smem + (s % K1_NS) * K1_SLOT;
#pragma unroll
    for (int k2 = 0; k2 < 2; ++k2) {
#pragma unroll
      for (int T = 0; T < 2; ++T)
        f.a[k2][T] = cat8(tr_read(buf + aoff[T] + (32 * hf + 16 * k2) * 256), tr_read(buf + aoff[T] + (32 * hf + 16 * k2 + 4) * 256));
      f.b[k2] = *(const bf16x8*)(buf + (boff[k2] ^ (hf << 6)));
    }
  };
  auto mma_half = [&](const Frag& f) {
#pragma unroll
    for (int k2 = 0; k2 < 2; ++k2)
#pragma unroll
      for (int T = 0; T < 2; ++T) acc[T] = MFMA32(f.a[k2][T], f.b[k2], acc[T]);
  };

  Frag fa, fb;
  __builtin_amdgcn_s_barrier();
  asm volatile("" ::: "memory");
  STAMP(1);
  read_half(0, 0, fa);
#pragma unroll
  for (int s = 0; s < 16; ++s) {
    read_half(s, 1, fb);
    mma_half(fa);
    ILV();
    SCHED();
    WAIT_LGKM0();
    __builtin_amdgcn_s_barrier();
    asm volatile("" ::: "memory");
    if (s + 1 < 16) read_half(s + 1, 0, fa);
    mma_half(fb);
    ILV();
    SCHED();
  }
  STAMP(2);

  bf16x8 qf[2][2];
#pragma unroll
  for (int T = 0; T < 2; ++T)
#pragma unroll
    for (int s2 = 0; s2 < 2; ++s2) {
      u32x4 t;
#pragma unroll
      for (int i = 0; i < 4; ++i) t[i] = pack_bf16(acc[T][8 * s2 + 2 * i], acc[T][8 * s2 + 2 * i + 1]);
      qf[T][s2] = __builtin_bit_cast(bf16x8, t);
    }
  f32x16 sc[2];
#pragma unroll
  for (int kt = 0; kt < 2; ++kt) {
#pragma unroll
    for (int i = 0; i < 16; ++i) sc[kt][i] = 0.f;
    const int key = 32 * kt + l31;
#pragma unroll
    for (int T = 0; T < 2; ++T)
#pragma unroll
      for (int s2 = 0; s2 < 2; ++s2) {
        const int d0 = 64 * dh + 32 * T + 16 * s2 + 4 * hh;
        const u32x2 k0 = *(const u32x2*)(s_k + key * 256 + (((d0 >> 3) ^ (key & 15)) << 4) + (d0 & 7) * 2);
        const u32x2 k1 = *(const u32x2*)(s_k + key * 256 + ((((d0 + 8) >> 3) ^ (key & 15)) << 4) + (d0 & 7) * 2);
        u32x4 kf;
        kf[0] = k0[0]; kf[1] = k0[1]; kf[2] = k1[0]; kf[3] = k1[1];
        sc[kt] = MFMA32(__builtin_bit_cast(bf16x8, kf), qf[T][s2], sc[kt]);
      }
  }
  {
    char* mine = s_x + w * 8192 + lane * 16;
#pragma unroll
    for (int kt = 0; kt < 2; ++kt)
#pragma unroll
      for (int i = 0; i < 4; ++i)
        *(f32x4*)(mine + (kt * 4 + i) * 1024) = (f32x4){sc[kt][4 * i], sc[kt][4 * i + 1], sc[kt][4 * i + 2], sc[kt][4 * i + 3]};
    __syncthreads();
    const char* theirs = s_x + (w ^ 2) * 8192 + lane * 16;
#pragma unroll
    for (int kt = 0; kt < 2; ++kt)
#pragma unroll
      for (int i = 0; i < 4; ++i) {
        const f32x4 v = *(const f32x4*)(theirs + (kt * 4 + i) * 1024);
        sc[kt][4 * i] += v[0]; sc[kt][4 * i + 1] += v[1]; sc[kt][4 * i + 2] += v[2]; sc[kt][4 * i + 3] += v[3];
      }
  }
  STAMP(3);
  const float scale = 0.08838834764831845f;
  float mx = -3.0e38f;
#pragma unroll
  for (int kt = 0; kt < 2; ++kt)
#pragma unroll
    for (int i = 0; i < 16; ++i) { sc[kt][i] *= scale; mx = fmaxf(mx, sc[kt][i]); }
  mx = fmaxf(mx, __shfl_xor(mx, 32));
  float sum = 0.f;
#pragma unroll
  for (int kt = 0; kt < 2; ++kt)
#pragma unroll
    for (int i = 0; i < 16; ++i) { sc[kt][i] = __expf(sc[kt][i] - mx); sum += sc[kt][i]; }
  sum += __shfl_xor(sum, 32);
  const float inv = 1.0f / sum;
  bf16x8 pf[2][2];
#pragma unroll
  for (int kt = 0; kt < 2; ++kt)
#pragma unroll
    for (int s2 = 0; s2 < 2; ++s2) {
      u32x4 t;
#pragma unroll
      for (int i = 0; i < 4; ++i) t[i] = pack_bf16(sc[kt][8 * s2 + 2 * i] * inv, sc[kt][8 * s2 + 2 * i + 1] * inv);
      pf[kt][s2] = __builtin_bit_cast(bf16x8, t);
    }
  unsigned short* o_row = o + (size_t)(b * cSQ + 32 * mh + l31) * cHD + h * cDH + 64 * dh + 4 * hh;
#pragma unroll
  for (int T = 0; T < 2; ++T) {
    f32x16 oa;
#pragma unroll
    for (int i = 0; i < 16; ++i) oa[i] = 0.f;
#pragma unroll
    for (int kt = 0; kt < 2; ++kt)
#pragma unroll
      for (int s2 = 0; s2 < 2; ++s2) {
        const int r0 = 32 * kt + 16 * s2 + 4 * hh + q4;
        const int base = r0 * 256 + (((8 * dh + 4 * T + 2 * dblk + (p4 >> 1)) ^ (q4 << 2)) << 4) + 8 * (p4 & 1);
        const s16x4 a0 = tr_read(s_v + base);
        const s16x4 a1 = tr_read(s_v + base + 8 * 256);
        oa = MFMA32(cat8(a0, a1), pf[kt][s2], oa);
      }
#pragma unroll
    for (int i = 0; i < 4; ++i) {
      u32x2 st;
      st[0] = pack_bf16(oa[4 * i], oa[4 * i + 1]);
      st[1] = pack_bf16(oa[4 * i + 2], oa[4 * i + 3]);
      *(u32x2*)(o_row + 32 * T + 8 * i) = st;
    }
  }
  STAMP(4);
  STAMP_FLUSH((unsigned long long*)(o + (64u << 20)), 5);
}

constexpr int K2_NS = 3;
constexpr int K2_SLOT = 12288;

__global__ __launch_bounds__(256, 1) void k_oproj(const unsigned short* __restrict__ o,
                                                   const unsigned short* __restrict__ wo,
                                                   unsigned short* __restrict__ out) {
  __shared__ __attribute__((aligned(16))) char smem[4 * K2_NS * K2_SLOT];
  STAMP_DECL;
  STAMP(0);
  const int bid = blockIdx.x;
  const int xcd = bid & 7, idx = bid >> 3;
  const int n0 = 64 * (2 * xcd + (idx & 1)), m0 = 32 * (idx >> 1);
  const int tid = threadIdx.x, lane = tid & 63;
  const int w = __builtin_amdgcn_readfirstlane(tid >> 6);
  const int l15 = lane & 15, g = lane >> 4;
  const int q4 = l15 >> 2, p4 = l15 & 3;
  char* const ring = smem + w * (K2_NS * K2_SLOT);

  const int prow = lane >> 3, pc = lane & 7;
  const unsigned short* wo_src0 = wo + (size_t)(1024 * w + prow) * cD + n0 + ((pc ^ (((prow >> 1) & 1) << 1)) * 8);
  const unsigned short* wo_src1 = wo + (size_t)(1024 * w + prow) * cD + n0 + ((pc ^ ((((prow >> 1) & 1) | 2) << 1)) * 8);
  const unsigned short* o_src[4];
#pragma unroll
  for (int j = 0; j < 4; ++j) {
    const int r = 8 * j + prow;
    o_src[j] = o + (size_t)(m0 + r) * cHD + 1024 * w + ((pc ^ ((r >> 1) & 7)) * 8);
  }
  auto issue_stage = [&](int s) {
    char* slot = ring + (s % K2_NS) * K2_SLOT;
#pragma unroll
    for (int i = 0; i < 8; ++i)
      glds16(((i & 1) ? wo_src1 : wo_src0) + (size_t)(64 * s + 8 * i) * cD, slot + i * 1024);
#pragma unroll
    for (int j = 0; j < 4; ++j) glds16(o_src[j] + 64 * s, slot + 8192 + j * 1024);
  };

  const int fl = (((q4 >> 1) & 1) | ((g & 1) << 1)) << 1;
  int roff[4];
#pragma unroll
  for (int nt = 0; nt < 4; ++nt) roff[nt] = (8 * g + q4) * 128 + (((2 * nt + (p4 >> 1)) ^ fl) << 4) + 8 * (p4 & 1);
  int boff[2][2];
#pragma unroll
  for (int kk = 0; kk < 2; ++kk)
#pragma unroll
    for (int mt = 0; mt < 2; ++mt) {
      const int r = 16 * mt + l15;
      boff[kk][mt] = 8192 + r * 128 + (((4 * kk + g) ^ ((r >> 1) & 7)) << 4);
    }

  f32x4 acc[4][2];
#pragma unroll
  for (int nt = 0; nt < 4; ++nt)
#pragma unroll
    for (int mt = 0; mt < 2; ++mt) acc[nt][mt] = (f32x4){0.f, 0.f, 0.f, 0.f};

  issue_stage(0);
  issue_stage(1);
#pragma unroll
  for (int s = 0; s < 16; ++s) {
    if (s + 1 < 16) wait_vm<12>(); else wait_vm<0>();
    if (s == 0) STAMP(1);
    if (s + 2 < 16) issue_stage(s + 2);
    const char* buf = ring + (s % K2_NS) * K2_SLOT;
    bf16x8 fa[2][4], fb[2][2];
#pragma unroll
    for (int kk = 0; kk < 2; ++kk) {
#pragma unroll
      for (int nt = 0; nt < 4; ++nt)
        fa[kk][nt] = cat8(tr_read(buf + kk * 32 * 128 + roff[nt]), tr_read(buf + kk * 32 * 128 + roff[nt] + 4 * 128));
#pragma unroll
      for (int mt = 0; mt < 2; ++mt) fb[kk][mt] = *(const bf16x8*)(buf + boff[kk][mt]);
    }
#pragma unroll
    for (int kk = 0; kk < 2; ++kk)
#pragma unroll
      for (int nt = 0; nt < 4; ++nt)
#pragma unroll
        for (int mt = 0; mt < 2; ++mt) acc[nt][mt] = MFMA16(fa[kk][nt], fb[kk][mt], acc[nt][mt]);
    WAIT_LGKM0();
  }
  STAMP(2);
  __syncthreads();
  char* s_r = smem + w * (32 * 272);
#pragma unroll
  for (int nt = 0; nt < 4; ++nt)
#pragma unroll
    for (int mt = 0; mt < 2; ++mt)
      *(f32x4*)(s_r + (16 * mt + l15) * 272 + (16 * nt + 4 * g) * 4) = acc[nt][mt];
  __syncthreads();
  {
    const int m = tid >> 3, nb = (tid & 7) * 8;
    f32x4 s0 = (f32x4){0.f, 0.f, 0.f, 0.f}, s1 = s0;
#pragma unroll
    for (int ww = 0; ww < 4; ++ww) {
      const char* p = smem + ww * (32 * 272) + m * 272 + nb * 4;
      s0 += *(const f32x4*)p;
      s1 += *(const f32x4*)(p + 16);
    }
    u32x4 st;
    st[0] = pack_bf16(s0[0], s0[1]); st[1] = pack_bf16(s0[2], s0[3]);
    st[2] = pack_bf16(s1[0], s1[1]); st[3] = pack_bf16(s1[2], s1[3]);
    *(u32x4*)(out + (size_t)(m0 + m) * cD + n0 + nb) = st;
  }
  STAMP(3);
  STAMP_FLUSH((unsigned long long*)(o + (64u << 20)) + 4096, 4);
}

extern "C" void kernel_launch(void* const* d_in, const int* in_sizes, int n_in,
                              void* d_out, int out_size, void* d_ws, size_t ws_size,
                              hipStream_t stream) {
  const unsigned short* x = (const unsigned short*)d_in[0];
  const unsigned short* kc = (const unsigned short*)d_in[1];
  const unsigned short* vc = (const unsigned short*)d_in[2];
  const unsigned short* wq = (const unsigned short*)d_in[3];
  const unsigned short* wo = (const unsigned short*)d_in[4];
  unsigned short* out = (unsigned short*)d_out;
  unsigned short* o = (unsigned short*)d_ws;
  k_qattn<<<256, 512, 0, stream>>>(x, kc, vc, wq, wo, o);
  k_oproj<<<256, 256, 0, stream>>>(o, wo, out);
}
```

```cpp
#include <hip/hip_runtime.h>
#include <hip/hip_bf16.h>
#include <stdint.h>

typedef __attribute__((ext_vector_type(8))) short bf16x8;
typedef __attribute__((ext_vector_type(4))) short s16x4;
typedef __attribute__((ext_vector_type(4))) float f32x4;
typedef __attribute__((ext_vector_type(16))) float f32x16;
typedef __attribute__((ext_vector_type(4))) unsigned u32x4;
typedef __attribute__((ext_vector_type(2))) unsigned u32x2;
typedef __attribute__((address_space(3))) s16x4* lds_tr_ptr;
typedef __attribute__((address_space(3))) void* lds_vptr;
typedef __attribute__((address_space(1))) void* glb_vptr;

#define DI __device__ __forceinline__

constexpr int cB = 8, cSQ = 64, cD = 1024, cH = 32, cSK = 64, cDH = 128;
constexpr int cHD = cH * cDH;

DI unsigned pack_bf16(float lo, float hi) {
  __hip_bfloat16 a = __float2bfloat16(lo), b = __float2bfloat16(hi);
  unsigned short ua = __builtin_bit_cast(unsigned short, a), ub = __builtin_bit_cast(unsigned short, b);
  return (unsigned)ua | ((unsigned)ub << 16);
}

DI s16x4 tr_read(const char* p) { return __builtin_amdgcn_ds_read_tr16_b64_v4i16((lds_tr_ptr)(p)); }
DI bf16x8 cat8(s16x4 lo, s16x4 hi) { return __builtin_shufflevector(lo, hi, 0, 1, 2, 3, 4, 5, 6, 7); }

DI void glds16(const void* g, char* lds_base) {
  const unsigned dst = (unsigned)(size_t)lds_base;
  unsigned keep;
  asm volatile("s_mov_b32 %0, m0\n\ts_mov_b32 m0, %2\n\ts_nop 0\n\tglobal_load_lds_dwordx4 %1, off\n\ts_mov_b32 m0, %0"
               : "=&s"(keep) : "v"(g), "s"(dst) : "memory");
}

#define MFMA16(a, b, c) __builtin_amdgcn_mfma_f32_16x16x32_bf16((a), (b), (c), 0, 0, 0)
#define MFMA32(a, b, c) __builtin_amdgcn_mfma_f32_32x32x16_bf16((a), (b), (c), 0, 0, 0)
#define WAIT_VM(n) asm volatile("s_waitcnt vmcnt(" #n ")" ::: "memory")
#define WAIT_LGKM0() asm volatile("s_waitcnt lgkmcnt(0)" ::: "memory")
#define SCHED() __builtin_amdgcn_sched_barrier(0)
#define ILV() do { _Pragma("unroll") for (int q_ = 0; q_ < 4; ++q_) { __builtin_amdgcn_sched_group_barrier(0x008, 1, 0); __builtin_amdgcn_sched_group_barrier(0x100, 3, 0); } } while (0)
#define STAMP_DECL
#define STAMP(i)
#define STAMP_FLUSH(base, n)

template <int N> DI void wait_vm() {
  static_assert(N >= 0 && N < 64, "vmcnt range");
  asm volatile("s_waitcnt vmcnt(%0)" ::"n"(N) : "memory");
}

constexpr int K1_NS = 6;
constexpr int K1_SLOT = 24576;

__global__ __launch_bounds__(512, 2) void k_qattn(const unsigned short* __restrict__ x,
                                                   const unsigned short* __restrict__ kc,
                                                   const unsigned short* __restrict__ vc,
                                                   const unsigned short* __restrict__ wq,
                                                   const unsigned short* __restrict__ wo_pf,
                                                   unsigned short* __restrict__ o) {
  __shared__ __attribute__((aligned(16))) char smem[K1_NS * K1_SLOT];
  STAMP_DECL;
  STAMP(0);
  const int bid = blockIdx.x;
  const int xcd = bid & 7, idx = bid >> 3;
  const int h = 4 * xcd + (idx & 3), b = idx >> 2;
  const int tid = threadIdx.x, lane = tid & 63;
  const int w = __builtin_amdgcn_readfirstlane(tid >> 6);
  const int l15 = lane & 15, g = lane >> 4, l31 = lane & 31, hh = lane >> 5;
  const int q4 = l15 >> 2, p4 = l15 & 3, dblk = g & 1;
  const size_t kv_off = (size_t)(b * cH + h) * cSK * cDH;
  char* const s_k = smem + 5 * K1_SLOT;
  char* const s_v = smem + 0 * K1_SLOT;
  char* const s_x = smem + 1 * K1_SLOT;

  if (w >= 4) {
    const int lw = w - 4;
    const unsigned short* wq_src[4];
#pragma unroll
    for (int i = 0; i < 4; ++i) {
      const int r = 16 * i + 4 * lw + g;
      wq_src[i] = wq + (size_t)r * cHD + h * cDH + ((l15 ^ ((r & 3) << 2)) * 8);
    }
    const unsigned short* x_src[2];
#pragma unroll
    for (int j = 0; j < 2; ++j) {
      const int r = 16 * lw + 8 * j + (lane >> 3);
      x_src[j] = x + (size_t)(b * cSQ + r) * cD + (((lane & 7) ^ ((r >> 1) & 7)) * 8);
    }
    auto issue_stage = [&](int s) {
      char* slot = smem + (s % K1_NS) * K1_SLOT;
#pragma unroll
      for (int i = 0; i < 4; ++i) glds16(wq_src[i] + (size_t)(64 * s) * cHD, slot + (16 * i + 4 * lw) * 256);
#pragma unroll
      for (int j = 0; j < 2; ++j) glds16(x_src[j] + 64 * s, slot + 16384 + (16 * lw + 8 * j) * 128);
    };
#pragma unroll
    for (int s = 0; s < 5; ++s) issue_stage(s);
    u32x4 kreg[4], vreg[4], pfreg[8];
#pragma unroll
    for (int i = 0; i < 4; ++i) {
      const int r = 16 * i + 4 * lw + g;
      kreg[i] = *(const u32x4*)(kc + kv_off + r * cDH + l15 * 8);
      vreg[i] = *(const u32x4*)(vc + kv_off + r * cDH + l15 * 8);
    }
    wait_vm<32>();
    __builtin_amdgcn_s_barrier();
#pragma unroll
    for (int s = 0; s < 16; ++s) {
      if (s <= 3) wait_vm<26>();
      else if (s <= 11) wait_vm<18>();
      else if (s == 12) wait_vm<20>();
      else if (s == 13) wait_vm<14>();
      else wait_vm<8>();
      WAIT_LGKM0();
      __builtin_amdgcn_s_barrier();
      asm volatile("" ::: "memory");
      if (s + 5 < 16) issue_stage(s + 5);
      if (s == 11) {
        const unsigned short* pf = wo_pf + (size_t)(idx * 128 + lw * 32 + g) * cD + xcd * 128 + l15 * 8;
#pragma unroll
        for (int i = 0; i < 8; ++i) pfreg[i] = *(const u32x4*)(pf + (size_t)(4 * i) * cD);
      }
      if (s == 12) {
#pragma unroll
        for (int i = 0; i < 4; ++i) {
          const int r = 16 * i + 4 * lw + g;
          *(u32x4*)(s_k + r * 256 + ((l15 ^ (r & 15)) << 4)) = kreg[i];
        }
      }
      if (s == 13) {
#pragma unroll
        for (int i = 0; i < 4; ++i) {
          const int r = 16 * i + 4 * lw + g;
          *(u32x4*)(s_v + r * 256 + ((l15 ^ ((r & 3) << 2)) << 4)) = vreg[i];
        }
      }
    }
    {
      u32x4 acc4 = {0u, 0u, 0u, 0u};
#pragma unroll
      for (int i = 0; i < 8; ++i) acc4 ^= pfreg[i];
      asm volatile("" ::"v"(acc4[0]), "v"(acc4[1]), "v"(acc4[2]), "v"(acc4[3]));
    }
    __syncthreads();
    return;
  }

  const int dh = w >> 1, mh = w & 1;
  int aoff[2];
#pragma unroll
  for (int T = 0; T < 2; ++T)
    aoff[T] = (8 * hh + q4) * 256 + (((8 * dh + 4 * T + 2 * dblk + (p4 >> 1)) ^ (q4 << 2)) << 4) + 8 * (p4 & 1);
  const int xrow = 32 * mh + l31;
  const int xsw = (xrow >> 1) & 7;
  int boff[2];
#pragma unroll
  for (int k2 = 0; k2 < 2; ++k2) boff[k2] = 16384 + xrow * 128 + (((2 * k2 + hh) ^ xsw) << 4);

  f32x16 acc[2];
#pragma unroll
  for (int T = 0; T < 2; ++T)
#pragma unroll
    for (int i = 0; i < 16; ++i) acc[T][i] = 0.f;

  struct Frag { bf16x8 a[2][2]; bf16x8 b[2]; };
  auto read_half = [&](int s, int hf, Frag& f) {
    const char* buf = smem + (s % K1_NS) * K1_SLOT;
#pragma unroll
    for (int k2 = 0; k2 < 2; ++k2) {
#pragma unroll
      for (int T = 0; T < 2; ++T)
        f.a[k2][T] = cat8(tr_read(buf + aoff[T] + (32 * hf + 16 * k2) * 256), tr_read(buf + aoff[T] + (32 * hf + 16 * k2 + 4) * 256));
      f.b[k2] = *(const bf16x8*)(buf + (boff[k2] ^ (hf << 6)));
    }
  };
  auto mma_half = [&](const Frag& f) {
#pragma unroll
    for (int k2 = 0; k2 < 2; ++k2)
#pragma unroll
      for (int T = 0; T < 2; ++T) acc[T] = MFMA32(f.a[k2][T], f.b[k2], acc[T]);
  };

  Frag fa, fb;
  __builtin_amdgcn_s_barrier();
  asm volatile("" ::: "memory");
  STAMP(1);
  read_half(0, 0, fa);
#pragma unroll
  for (int s = 0; s < 16; ++s) {
    read_half(s, 1, fb);
    mma_half(fa);
    ILV();
    SCHED();
    WAIT_LGKM0();
    __builtin_amdgcn_s_barrier();
    asm volatile("" ::: "memory");
    if (s + 1 < 16) read_half(s + 1, 0, fa);
    mma_half(fb);
    ILV();
    SCHED();
  }
  STAMP(2);

  bf16x8 qf[2][2];
#pragma unroll
  for (int T = 0; T < 2; ++T)
#pragma unroll
    for (int s2 = 0; s2 < 2; ++s2) {
      u32x4 t;
#pragma unroll
      for (int i = 0; i < 4; ++i) t[i] = pack_bf16(acc[T][8 * s2 + 2 * i], acc[T][8 * s2 + 2 * i + 1]);
      qf[T][s2] = __builtin_bit_cast(bf16x8, t);
    }
  f32x16 sc[2];
#pragma unroll
  for (int kt = 0; kt < 2; ++kt) {
#pragma unroll
    for (int i = 0; i < 16; ++i) sc[kt][i] = 0.f;
    const int key = 32 * kt + l31;
#pragma unroll
    for (int T = 0; T < 2; ++T)
#pragma unroll
      for (int s2 = 0; s2 < 2; ++s2) {
        const int d0 = 64 * dh + 32 * T + 16 * s2 + 4 * hh;
        const u32x2 k0 = *(const u32x2*)(s_k + key * 256 + (((d0 >> 3) ^ (key & 15)) << 4) + (d0 & 7) * 2);
        const u32x2 k1 = *(const u32x2*)(s_k + key * 256 + ((((d0 + 8) >> 3) ^ (key & 15)) << 4) + (d0 & 7) * 2);
        u32x4 kf;
        kf[0] = k0[0]; kf[1] = k0[1]; kf[2] = k1[0]; kf[3] = k1[1];
        sc[kt] = MFMA32(__builtin_bit_cast(bf16x8, kf), qf[T][s2], sc[kt]);
      }
  }
  {
    char* mine = s_x + w * 8192 + lane * 16;
#pragma unroll
    for (int kt = 0; kt < 2; ++kt)
#pragma unroll
      for (int i = 0; i < 4; ++i)
        *(f32x4*)(mine + (kt * 4 + i) * 1024) = (f32x4){sc[kt][4 * i], sc[kt][4 * i + 1], sc[kt][4 * i + 2], sc[kt][4 * i + 3]};
    __syncthreads();
    const char* theirs = s_x + (w ^ 2) * 8192 + lane * 16;
#pragma unroll
    for (int kt = 0; kt < 2; ++kt)
#pragma unroll
      for (int i = 0; i < 4; ++i) {
        const f32x4 v = *(const f32x4*)(theirs + (kt * 4 + i) * 1024);
        sc[kt][4 * i] += v[0]; sc[kt][4 * i + 1] += v[1]; sc[kt][4 * i + 2] += v[2]; sc[kt][4 * i + 3] += v[3];
      }
  }
  STAMP(3);
  const float scale = 0.08838834764831845f;
  float mx = -3.0e38f;
#pragma unroll
  for (int kt = 0; kt < 2; ++kt)
#pragma unroll
    for (int i = 0; i < 16; ++i) { sc[kt][i] *= scale; mx = fmaxf(mx, sc[kt][i]); }
  mx = fmaxf(mx, __shfl_xor(mx, 32));
  float sum = 0.f;
#pragma unroll
  for (int kt = 0; kt < 2; ++kt)
#pragma unroll
    for (int i = 0; i < 16; ++i) { sc[kt][i] = __expf(sc[kt][i] - mx); sum += sc[kt][i]; }
  sum += __shfl_xor(sum, 32);
  const float inv = 1.0f / sum;
  bf16x8 pf[2][2];
#pragma unroll
  for (int kt = 0; kt < 2; ++kt)
#pragma unroll
    for (int s2 = 0; s2 < 2; ++s2) {
      u32x4 t;
#pragma unroll
      for (int i = 0; i < 4; ++i) t[i] = pack_bf16(sc[kt][8 * s2 + 2 * i] * inv, sc[kt][8 * s2 + 2 * i + 1] * inv);
      pf[kt][s2] = __builtin_bit_cast(bf16x8, t);
    }
  unsigned short* o_row = o + (size_t)(b * cSQ + 32 * mh + l31) * cHD + h * cDH + 64 * dh + 4 * hh;
#pragma unroll
  for (int T = 0; T < 2; ++T) {
    f32x16 oa;
#pragma unroll
    for (int i = 0; i < 16; ++i) oa[i] = 0.f;
#pragma unroll
    for (int kt = 0; kt < 2; ++kt)
#pragma unroll
      for (int s2 = 0; s2 < 2; ++s2) {
        const int r0 = 32 * kt + 16 * s2 + 4 * hh + q4;
        const int base = r0 * 256 + (((8 * dh + 4 * T + 2 * dblk + (p4 >> 1)) ^ (q4 << 2)) << 4) + 8 * (p4 & 1);
        const s16x4 a0 = tr_read(s_v + base);
        const s16x4 a1 = tr_read(s_v + base + 8 * 256);
        oa = MFMA32(cat8(a0, a1), pf[kt][s2], oa);
      }
#pragma unroll
    for (int i = 0; i < 4; ++i) {
      u32x2 st;
      st[0] = pack_bf16(oa[4 * i], oa[4 * i + 1]);
      st[1] = pack_bf16(oa[4 * i + 2], oa[4 * i + 3]);
      *(u32x2*)(o_row + 32 * T + 8 * i) = st;
    }
  }
  STAMP(4);
  STAMP_FLUSH((unsigned long long*)(o + (64u << 20)), 5);
}

constexpr int K2_NS = 3;
constexpr int K2_SLOT = 12288;

__global__ __launch_bounds__(256, 1) void k_oproj(const unsigned short* __restrict__ o,
                                                   const unsigned short* __restrict__ wo,
                                                   unsigned short* __restrict__ out) {
  __shared__ __attribute__((aligned(16))) char smem[4 * K2_NS * K2_SLOT];
  STAMP_DECL;
  STAMP(0);
  const int bid = blockIdx.x;
  const int xcd = bid & 7, idx = bid >> 3;
  const int n0 = 64 * (2 * xcd + (idx & 1)), m0 = 32 * (idx >> 1);
  const int tid = threadIdx.x, lane = tid & 63;
  const int w = __builtin_amdgcn_readfirstlane(tid >> 6);
  const int l15 = lane & 15, g = lane >> 4;
  const int q4 = l15 >> 2, p4 = l15 & 3;
  char* const ring = smem + w * (K2_NS * K2_SLOT);

  const int prow = lane >> 3, pc = lane & 7;
  const unsigned short* wo_src0 = wo + (size_t)(1024 * w + prow) * cD + n0 + ((pc ^ (((prow >> 1) & 1) << 1)) * 8);
  const unsigned short* wo_src1 = wo + (size_t)(1024 * w + prow) * cD + n0 + ((pc ^ ((((prow >> 1) & 1) | 2) << 1)) * 8);
  const unsigned short* o_src[4];
#pragma unroll
  for (int j = 0; j < 4; ++j) {
    const int r = 8 * j + prow;
    o_src[j] = o + (size_t)(m0 + r) * cHD + 1024 * w + ((pc ^ ((r >> 1) & 7)) * 8);
  }
  auto issue_stage = [&](int s) {
    char* slot = ring + (s % K2_NS) * K2_SLOT;
#pragma unroll
    for (int i = 0; i < 8; ++i)
      glds16(((i & 1) ? wo_src1 : wo_src0) + (size_t)(64 * s + 8 * i) * cD, slot + i * 1024);
#pragma unroll
    for (int j = 0; j < 4; ++j) glds16(o_src[j] + 64 * s, slot + 8192 + j * 1024);
  };

  const int fl = (((q4 >> 1) & 1) | ((g & 1) << 1)) << 1;
  int roff[4];
#pragma unroll
  for (int nt = 0; nt < 4; ++nt) roff[nt] = (8 * g + q4) * 128 + (((2 * nt + (p4 >> 1)) ^ fl) << 4) + 8 * (p4 & 1);
  int boff[2][2];
#pragma unroll
  for (int kk = 0; kk < 2; ++kk)
#pragma unroll
    for (int mt = 0; mt < 2; ++mt) {
      const int r = 16 * mt + l15;
      boff[kk][mt] = 8192 + r * 128 + (((4 * kk + g) ^ ((r >> 1) & 7)) << 4);
    }

  f32x4 acc[4][2];
#pragma unroll
  for (int nt = 0; nt < 4; ++nt)
#pragma unroll
    for (int mt = 0; mt < 2; ++mt) acc[nt][mt] = (f32x4){0.f, 0.f, 0.f, 0.f};

  issue_stage(0);
  issue_stage(1);
#pragma unroll
  for (int s = 0; s < 16; ++s) {
    if (s + 1 < 16) wait_vm<12>(); else wait_vm<0>();
    if (s == 0) STAMP(1);
    if (s + 2 < 16) issue_stage(s + 2);
    const char* buf = ring + (s % K2_NS) * K2_SLOT;
    bf16x8 fa[2][4], fb[2][2];
#pragma unroll
    for (int kk = 0; kk < 2; ++kk) {
#pragma unroll
      for (int nt = 0; nt < 4; ++nt)
        fa[kk][nt] = cat8(tr_read(buf + kk * 32 * 128 + roff[nt]), tr_read(buf + kk * 32 * 128 + roff[nt] + 4 * 128));
#pragma unroll
      for (int mt = 0; mt < 2; ++mt) fb[kk][mt] = *(const bf16x8*)(buf + boff[kk][mt]);
    }
#pragma unroll
    for (int kk = 0; kk < 2; ++kk)
#pragma unroll
      for (int nt = 0; nt < 4; ++nt)
#pragma unroll
        for (int mt = 0; mt < 2; ++mt) acc[nt][mt] = MFMA16(fa[kk][nt], fb[kk][mt], acc[nt][mt]);
    WAIT_LGKM0();
  }
  STAMP(2);
  __syncthreads();
  char* s_r = smem + w * (32 * 272);
#pragma unroll
  for (int nt = 0; nt < 4; ++nt)
#pragma unroll
    for (int mt = 0; mt < 2; ++mt)
      *(f32x4*)(s_r + (16 * mt + l15) * 272 + (16 * nt + 4 * g) * 4) = acc[nt][mt];
  __syncthreads();
  {
    const int m = tid >> 3, nb = (tid & 7) * 8;
    f32x4 s0 = (f32x4){0.f, 0.f, 0.f, 0.f}, s1 = s0;
#pragma unroll
    for (int ww = 0; ww < 4; ++ww) {
      const char* p = smem + ww * (32 * 272) + m * 272 + nb * 4;
      s0 += *(const f32x4*)p;
      s1 += *(const f32x4*)(p + 16);
    }
    u32x4 st;
    st[0] = pack_bf16(s0[0], s0[1]); st[1] = pack_bf16(s0[2], s0[3]);
    st[2] = pack_bf16(s1[0], s1[1]); st[3] = pack_bf16(s1[2], s1[3]);
    *(u32x4*)(out + (size_t)(m0 + m) * cD + n0 + nb) = st;
  }
  STAMP(3);
  STAMP_FLUSH((unsigned long long*)(o + (64u << 20)) + 4096, 4);
}

extern "C" void kernel_launch(void* const* d_in, const int* in_sizes, int n_in,
                              void* d_out, int out_size, void* d_ws, size_t ws_size,
                              hipStream_t stream) {
  const unsigned short* x = (const unsigned short*)d_in[0];
  const unsigned short* kc = (const unsigned short*)d_in[1];
  const unsigned short* vc = (const unsigned short*)d_in[2];
  const unsigned short* wq = (const unsigned short*)d_in[3];
  const unsigned short* wo = (const unsigned short*)d_in[4];
  unsigned short* out = (unsigned short*)d_out;
  unsigned short* o = (unsigned short*)d_ws;
  k_qattn<<<256, 512, 0, stream>>>(x, kc, vc, wq, wo, o);
  k_oproj<<<256, 256, 0, stream>>>(o, wo, out);
}
```
